```python
import torch

def _softmax(x, axis=-1):
    return torch.softmax(x, dim=axis)
def _gelu(x):
    return torch.nn.functional.gelu(x, approximate="tanh")
def _silu(x):
    return torch.nn.functional.silu(x)
def _sigmoid(x):
    return torch.sigmoid(x)
def _layernorm(x, axis=-1, eps=1e-5):
    m = x.mean(dim=axis, keepdim=True)
    v = x.var(dim=axis, keepdim=True, unbiased=False)
    return (x - m) / torch.sqrt(v + eps)
def _scatter_add(tgt, idx, src, axis=0):
    return tgt.clone().index_add_(axis, idx, src)
def _segment_sum(src, seg, n):
    out = torch.zeros((n,) + src.shape[1:], dtype=src.dtype)
    return out.index_add_(0, seg, src)
def _pad(x, pw):
    flat = tuple(v for pair in reversed(pw) for v in pair)
    return torch.nn.functional.pad(x, flat)

kernel_name = 'sequential_preLN__mha_block_cached__swiglu_ffn__decode'

def setup_inputs():

    _w = (lambda a: (lambda t: t.to(torch.bfloat16) if t.is_floating_point() else t)(torch.as_tensor(a)))
    def _A_setup():
        B, Sq, D = 8, 1, 512
        H, Sk, Dh = 32, 4096, 128
        return {
            "x": _w((lambda *s: torch.randn(*s, dtype=torch.bfloat16) * 0.05)(B, Sq, D)),
            "k_cache": _w((lambda *s: torch.randn(*s, dtype=torch.bfloat16) * 3.2)(B, H, Sk, Dh)),
            "v_cache": _w((lambda *s: torch.randn(*s, dtype=torch.bfloat16) * 0.4)(B, H, Sk, Dh)),
            "wq": _w((lambda *s: torch.randn(*s, dtype=torch.bfloat16) * 3.2)(D, H * Dh) * (D ** -0.5)),
            "wo": _w((lambda *s: torch.randn(*s, dtype=torch.bfloat16) * 0.4)(H * Dh, D) * ((H * Dh) ** -0.5)),
        }
    _A_in = _A_setup()

    def _B_setup():
        B, S, D, F = 8, 1, 512, 1376
        return {
            "x": _w((lambda *s: torch.randn(*s, dtype=torch.bfloat16) * 0.05)(B, S, D)),
            "w_gate": _w((lambda *s: torch.randn(*s, dtype=torch.bfloat16) * 0.05)(D, F)),
            "w_up": _w((lambda *s: torch.randn(*s, dtype=torch.bfloat16) * 0.05)(D, F)),
            "w_down": _w((lambda *s: torch.randn(*s, dtype=torch.bfloat16) * 0.05)(F, D)),
        }
    _B_in = _B_setup()

    return {
        "A_x": _A_in["x"],
        "A_k_cache": _A_in["k_cache"],
        "A_v_cache": _A_in["v_cache"],
        "A_wq": _A_in["wq"],
        "A_wo": _A_in["wo"],
        "B_x": _B_in["x"],
        "B_w_gate": _B_in["w_gate"],
        "B_w_up": _B_in["w_up"],
        "B_w_down": _B_in["w_down"],
    }

def reference(A_x, A_k_cache, A_v_cache, A_wq, A_wo, B_x, B_w_gate, B_w_up, B_w_down):
    def _A_ref(x, k_cache, v_cache, wq, wo):
        B, Sq, D = x.shape
        H, Dh = 32, 128
        q = (lambda x, *d: x.permute(*d))((x @ wq).reshape(B, Sq, H, Dh), 0, 2, 1, 3)
        scores = torch.einsum("bhqd,bhkd->bhqk", q, k_cache) * (Dh ** -0.5)
        p = _softmax(scores, axis=-1)
        o = torch.einsum("bhqk,bhkd->bhqd", p, v_cache)
        return (lambda x, *d: x.permute(*d))(o, 0, 2, 1, 3).reshape(B, Sq, H * Dh) @ wo

    def _B_ref(x, w_gate, w_up, w_down):
        g = _silu(x @ w_gate)
        u = x @ w_up
        return (g * u) @ w_down

    x = A_x
    h = x + _A_ref(_layernorm(x, axis=-1), A_k_cache, A_v_cache, A_wq, A_wo)
    h2 = h + _B_ref(_layernorm(h, axis=-1), B_w_gate, B_w_up, B_w_down)
    return h2
```

```cpp
#include <hip/hip_runtime.h>
#include <hip/hip_bf16.h>
#include <cstdint>

typedef __attribute__((ext_vector_type(4))) unsigned int u32x4;
typedef __attribute__((ext_vector_type(2))) unsigned int u32x2;
typedef __attribute__((ext_vector_type(4))) float f32x4;
typedef __attribute__((ext_vector_type(2))) __bf16 bf16x2;

constexpr int B_ = 8, D_ = 512, H_ = 32, SK = 4096, DH = 128, F_ = 1376;
constexpr int NW = 16;
#define RING 8
constexpr int U = RING;
constexpr int NIT = SK / (NW * U * 4);

__device__ __forceinline__ float bflo(unsigned v) { return __uint_as_float(v << 16); }
__device__ __forceinline__ float bfhi(unsigned v) { return __uint_as_float(v & 0xffff0000u); }
__device__ __forceinline__ float rbf(float f) { __bf16 b = (__bf16)f; return (float)b; }
__device__ __forceinline__ unsigned short f2bf(float f) { __bf16 b = (__bf16)f; return __builtin_bit_cast(unsigned short, b); }
__device__ __forceinline__ unsigned pack2(float lo, float hi) { return (unsigned)f2bf(lo) | ((unsigned)f2bf(hi) << 16); }
__device__ __forceinline__ float dot2(unsigned a, unsigned b, float acc) {
  return __builtin_amdgcn_fdot2_f32_bf16(__builtin_bit_cast(bf16x2, a), __builtin_bit_cast(bf16x2, b), acc, false);
}
template <int CTRL> __device__ __forceinline__ float dpp(float x) {
  return __builtin_bit_cast(float, __builtin_amdgcn_mov_dpp(__builtin_bit_cast(int, x), CTRL, 0xf, 0xf, true));
}
constexpr int XOR1 = 0xB1, XOR2 = 0x4E, XOR7 = 0x141, XOR8 = 0x128, ROR4 = 0x124;
__device__ __forceinline__ float xrow16_max(float x) {
  auto s = __builtin_amdgcn_permlane16_swap(__float_as_uint(x), __float_as_uint(x), false, false);
  x = fmaxf(__uint_as_float(s[0]), __uint_as_float(s[1]));
  auto t = __builtin_amdgcn_permlane32_swap(__float_as_uint(x), __float_as_uint(x), false, false);
  return fmaxf(__uint_as_float(t[0]), __uint_as_float(t[1]));
}
__device__ __forceinline__ float xrow16_sum(float x) {
  auto s = __builtin_amdgcn_permlane16_swap(__float_as_uint(x), __float_as_uint(x), false, false);
  x = __uint_as_float(s[0]) + __uint_as_float(s[1]);
  auto t = __builtin_amdgcn_permlane32_swap(__float_as_uint(x), __float_as_uint(x), false, false);
  return __uint_as_float(t[0]) + __uint_as_float(t[1]);
}
__device__ __forceinline__ float row16_sum(float x) {
  x += dpp<XOR8>(x); x += dpp<XOR7>(x); x += dpp<XOR2>(x); x += dpp<XOR1>(x); return x;
}
__device__ __forceinline__ float row16_max(float x) {
  x = fmaxf(x, dpp<XOR8>(x)); x = fmaxf(x, dpp<XOR7>(x)); x = fmaxf(x, dpp<XOR2>(x)); x = fmaxf(x, dpp<XOR1>(x)); return x;
}
__device__ __forceinline__ float wave_sum(float x) { return xrow16_sum(row16_sum(x)); }
__device__ __forceinline__ float wave_max(float x) { return xrow16_max(row16_max(x)); }
__device__ __forceinline__ u32x4 ldnt(const u32x4* p) { return __builtin_nontemporal_load(p); }

__device__ __forceinline__ void ln_stats(const float (&v)[8], float& m_bf, float& sd_bf) {
  float s = 0.f;
#pragma unroll
  for (int i = 0; i < 8; ++i) s += v[i];
  s = wave_sum(s);
  const float mean = s * (1.0f / 512.0f);
  float q = 0.f;
#pragma unroll
  for (int i = 0; i < 8; ++i) { const float d = v[i] - mean; q = fmaf(d, d, q); }
  q = wave_sum(q);
  const float var = rbf(q * (1.0f / 512.0f));
  m_bf = rbf(mean);
  sd_bf = rbf(sqrtf(rbf(var + 1e-5f)));
}

struct Lds {
  float red[NW * 512];
  unsigned short p[SK];
  unsigned q[64];
  float wmax[NW];
  float wsum[NW];
};

__global__ void __launch_bounds__(1024) attn_kernel(const unsigned short* __restrict__ x, const unsigned short* __restrict__ kc,
                                                    const unsigned short* __restrict__ vc, const unsigned short* __restrict__ wq,
                                                    const unsigned short* __restrict__ wo, float* __restrict__ partial) {
  __shared__ __attribute__((aligned(16))) Lds L;
  const int bid = blockIdx.x;
  const int b = bid >> 5, h = (bid & 7) * 4 + ((bid >> 3) & 3);
  const int tid = threadIdx.x, lane = tid & 63, w = __builtin_amdgcn_readfirstlane(tid >> 6), r = lane >> 4, c = lane & 15;
  const size_t bh = (size_t)b * H_ + h;
  const u32x4* kp = reinterpret_cast<const u32x4*>(kc + bh * SK * DH);
  const u32x4* vp = reinterpret_cast<const u32x4*>(vc + bh * SK * DH);

  const u32x4 xs = *reinterpret_cast<const u32x4*>(x + b * D_ + lane * 8);
  const u32x4 xr = *reinterpret_cast<const u32x4*>(x + b * D_ + w * 32 + r * 8);
  u32x4 wqv[8];
#pragma unroll
  for (int i = 0; i < 8; ++i)
    wqv[i] = *reinterpret_cast<const u32x4*>(wq + (size_t)(w * 32 + r * 8 + i) * (H_ * DH) + h * DH + c * 8);
  u32x4 buf[U];
#pragma unroll
  for (int u = 0; u < U; ++u) buf[u] = ldnt(kp + (size_t)w * U * 64 + u * 64 + lane);

  float xv[8];
#pragma unroll
  for (int k = 0; k < 4; ++k) { xv[2 * k] = bflo(xs[k]); xv[2 * k + 1] = bfhi(xs[k]); }
  float m_bf, sd_bf;
  ln_stats(xv, m_bf, sd_bf);
  float ln[8];
#pragma unroll
  for (int k = 0; k < 4; ++k) { ln[2 * k] = rbf(rbf(bflo(xr[k]) - m_bf) / sd_bf); ln[2 * k + 1] = rbf(rbf(bfhi(xr[k]) - m_bf) / sd_bf); }
  {
    float qa[8];
#pragma unroll
    for (int j = 0; j < 8; ++j) qa[j] = 0.f;
#pragma unroll
    for (int i = 0; i < 8; ++i)
#pragma unroll
      for (int k = 0; k < 4; ++k) { qa[2 * k] = fmaf(ln[i], bflo(wqv[i][k]), qa[2 * k]); qa[2 * k + 1] = fmaf(ln[i], bfhi(wqv[i][k]), qa[2 * k + 1]); }
#pragma unroll
    for (int j = 0; j < 8; ++j) qa[j] = xrow16_sum(qa[j]);
    if (r == 0) {
      *reinterpret_cast<f32x4*>(&L.red[w * 128 + c * 8]) = f32x4{qa[0], qa[1], qa[2], qa[3]};
      *reinterpret_cast<f32x4*>(&L.red[w * 128 + c * 8 + 4]) = f32x4{qa[4], qa[5], qa[6], qa[7]};
    }
  }
  __syncthreads();
  if (tid < 64) {
    float s0 = 0.f, s1 = 0.f;
#pragma unroll
    for (int i = 0; i < NW; ++i) { s0 += L.red[i * 128 + 2 * tid]; s1 += L.red[i * 128 + 2 * tid + 1]; }
    L.q[tid] = pack2(s0, s1);
  }
  __syncthreads();
  const u32x4 q = *reinterpret_cast<const u32x4*>(&L.q[c * 4]);

  constexpr int NS = (NIT * U) / 16;
  float sc[NS];
#pragma unroll
  for (int i = 0; i < NS; ++i) sc[i] = 0.f;
  float lmax = -INFINITY;
  float* scf = L.red;
  __syncthreads();
  for (int it = 0; it < NIT; ++it) {
    const u32x4* nxt = (it + 1 < NIT) ? kp + (size_t)((it + 1) * NW + w) * U * 64 + lane : vp + (size_t)w * U * 64 + lane;
    float xx[U];
#pragma unroll
    for (int u = 0; u < U; ++u) {
      float d = 0.f;
#pragma unroll
      for (int k = 0; k < 4; ++k) d = dot2(buf[u][k], q[k], d);
      xx[u] = d;
      buf[u] = ldnt(nxt + u * 64);
    }
#define TR_STEP(HALF, CTRL, BIT) _Pragma("unroll") for (int hh = 0; hh < (HALF); ++hh) { \
      const float lo_ = xx[hh], hi_ = xx[hh + (HALF)]; const float t1_ = lo_ + dpp<CTRL>(lo_), t2_ = hi_ + dpp<CTRL>(hi_); \
      xx[hh] = (c & (BIT)) ? t2_ : t1_; }
    int uidx;
    if constexpr (U == 16) { TR_STEP(8, XOR8, 8) TR_STEP(4, XOR7, 4) TR_STEP(2, XOR2, 2) TR_STEP(1, XOR1, 1) uidx = c; }
    else if constexpr (U == 8) { TR_STEP(4, XOR8, 8) TR_STEP(2, XOR7, 4) TR_STEP(1, XOR2, 2) xx[0] += dpp<XOR1>(xx[0]); uidx = c >> 1; }
    else { TR_STEP(2, XOR8, 8) TR_STEP(1, XOR7, 4) xx[0] += dpp<XOR2>(xx[0]); xx[0] += dpp<XOR1>(xx[0]); uidx = c >> 2; }
#undef TR_STEP
    const float s = rbf(rbf(xx[0]) * 0.08838834764831845f);
    lmax = fmaxf(lmax, s);
    scf[(it * NW + w) * (4 * U) + r * U + uidx] = s;
  }
  lmax = wave_max(lmax);
  if (lane == 0) L.wmax[w] = lmax;
  __syncthreads();
  float M = L.wmax[0];
#pragma unroll
  for (int i = 1; i < NW; ++i) M = fmaxf(M, L.wmax[i]);
  float e[4];
  {
    constexpr int PER = 4 * U;
    const int idx = lane * 4;
    const int it = idx / PER, off = idx % PER;
    const f32x4 sv = *reinterpret_cast<const f32x4*>(&scf[(it * NW + w) * PER + off]);
    float ls = 0.f;
#pragma unroll
    for (int i = 0; i < 4; ++i) { e[i] = __expf(sv[i] - M); ls += e[i]; }
    ls = wave_sum(ls);
    if (lane == 0) L.wsum[w] = ls;
    __syncthreads();
    float T = 0.f;
#pragma unroll
    for (int i = 0; i < NW; ++i) T += L.wsum[i];
    u32x2 pk; pk[0] = pack2(e[0] / T, e[1] / T); pk[1] = pack2(e[2] / T, e[3] / T);
    *reinterpret_cast<u32x2*>(&L.p[(it * NW + w) * PER + off]) = pk;
  }
  float acc[8];
#pragma unroll
  for (int j = 0; j < 8; ++j) acc[j] = 0.f;
  const u32x4* wop = reinterpret_cast<const u32x4*>(wo + (size_t)(h * DH) * D_);
  for (int it = 0; it < NIT; ++it) {
    unsigned pp[U / 2];
    {
      const unsigned* src = reinterpret_cast<const unsigned*>(&L.p[(it * NW + w) * (4 * U) + r * U]);
#pragma unroll
      for (int i = 0; i < U / 2; i += 2) { const u32x2 t = *reinterpret_cast<const u32x2*>(src + i); pp[i] = t[0]; pp[i + 1] = t[1]; }
    }
    const bool last = (it + 1 == NIT);
    const u32x4* nxt = last ? wop + (size_t)w * 8 * 64 + lane : vp + (size_t)((it + 1) * NW + w) * U * 64 + lane;
#pragma unroll
    for (int u = 0; u < U; u += 2) {
#pragma unroll
      for (int k = 0; k < 4; ++k) {
        const unsigned lo = __builtin_amdgcn_perm(buf[u + 1][k], buf[u][k], 0x05040100u);
        const unsigned hi = __builtin_amdgcn_perm(buf[u + 1][k], buf[u][k], 0x07060302u);
        acc[2 * k] = dot2(lo, pp[u / 2], acc[2 * k]);
        acc[2 * k + 1] = dot2(hi, pp[u / 2], acc[2 * k + 1]);
      }
      if (!last || u < 8) { buf[u] = ldnt(nxt + u * 64); buf[u + 1] = ldnt(nxt + (u + 1) * 64); }
    }
  }
#pragma unroll
  for (int j = 0; j < 8; ++j) acc[j] = xrow16_sum(acc[j]);
  __syncthreads();
  if (r == 0) {
    *reinterpret_cast<f32x4*>(&L.red[w * 128 + c * 8]) = f32x4{acc[0], acc[1], acc[2], acc[3]};
    *reinterpret_cast<f32x4*>(&L.red[w * 128 + c * 8 + 4]) = f32x4{acc[4], acc[5], acc[6], acc[7]};
  }
  __syncthreads();
  float ov;
  {
    const int d = 8 * w + (lane & 7), part = lane >> 3;
    ov = L.red[(2 * part) * 128 + d] + L.red[(2 * part + 1) * 128 + d];
    ov += dpp<XOR8>(ov);
    ov = xrow16_sum(ov);
    ov = rbf(ov);
  }
  __syncthreads();
  {
    float a2[8];
#pragma unroll
    for (int j = 0; j < 8; ++j) a2[j] = 0.f;
#pragma unroll
    for (int i = 0; i < 8; ++i) {
      const float o = __builtin_bit_cast(float, __builtin_amdgcn_readlane(__builtin_bit_cast(int, ov), i));
#pragma unroll
      for (int k = 0; k < 4; ++k) { a2[2 * k] = fmaf(o, bflo(buf[i][k]), a2[2 * k]); a2[2 * k + 1] = fmaf(o, bfhi(buf[i][k]), a2[2 * k + 1]); }
    }
    *reinterpret_cast<f32x4*>(&L.red[w * 512 + lane * 8]) = f32x4{a2[0], a2[1], a2[2], a2[3]};
    *reinterpret_cast<f32x4*>(&L.red[w * 512 + lane * 8 + 4]) = f32x4{a2[4], a2[5], a2[6], a2[7]};
  }
  __syncthreads();
  if (tid < 512) {
    float s = 0.f;
#pragma unroll
    for (int i = 0; i < NW; ++i) s += L.red[i * 512 + tid];
    partial[bh * 512 + tid] = s;
  }
}

__global__ void __launch_bounds__(64) reduce_ln_kernel(const unsigned short* __restrict__ x, const float* __restrict__ partial,
                                                       float* __restrict__ hbuf, unsigned short* __restrict__ hn) {
  const int b = blockIdx.x, lane = threadIdx.x;
  float a[8];
#pragma unroll
  for (int j = 0; j < 8; ++j) a[j] = 0.f;
  for (int h = 0; h < H_; ++h) {
    const f32x4 v0 = *reinterpret_cast<const f32x4*>(partial + ((size_t)b * H_ + h) * 512 + lane * 8);
    const f32x4 v1 = *reinterpret_cast<const f32x4*>(partial + ((size_t)b * H_ + h) * 512 + lane * 8 + 4);
#pragma unroll
    for (int j = 0; j < 4; ++j) { a[j] += v0[j]; a[4 + j] += v1[j]; }
  }
  const u32x4 xs = *reinterpret_cast<const u32x4*>(x + b * D_ + lane * 8);
  float hv[8];
#pragma unroll
  for (int k = 0; k < 4; ++k) { hv[2 * k] = rbf(bflo(xs[k]) + rbf(a[2 * k])); hv[2 * k + 1] = rbf(bfhi(xs[k]) + rbf(a[2 * k + 1])); }
  float m_bf, sd_bf;
  ln_stats(hv, m_bf, sd_bf);
#pragma unroll
  for (int j = 0; j < 8; ++j) { hbuf[b * 512 + lane * 8 + j] = hv[j]; hn[b * 512 + lane * 8 + j] = f2bf(rbf(hv[j] - m_bf) / sd_bf); }
}

__global__ void __launch_bounds__(512) gateup_kernel(const unsigned short* __restrict__ hn, const unsigned short* __restrict__ wg,
                                                     const unsigned short* __restrict__ wu, unsigned short* __restrict__ gu) {
  __shared__ float red[8][128];
  const int j0 = blockIdx.x * 8, d = threadIdx.x, lane = d & 63, w = d >> 6;
  const u32x4 g = *reinterpret_cast<const u32x4*>(wg + (size_t)d * F_ + j0);
  const u32x4 u = *reinterpret_cast<const u32x4*>(wu + (size_t)d * F_ + j0);
  for (int b = 0; b < 8; ++b) {
    const float hv = bflo(hn[b * 512 + d]);
#pragma unroll
    for (int k = 0; k < 4; ++k) {
      float t0 = wave_sum(hv * bflo(g[k])), t1 = wave_sum(hv * bfhi(g[k])), t2 = wave_sum(hv * bflo(u[k])), t3 = wave_sum(hv * bfhi(u[k]));
      if (lane == 0) { red[w][b * 16 + 2 * k] = t0; red[w][b * 16 + 2 * k + 1] = t1; red[w][b * 16 + 8 + 2 * k] = t2; red[w][b * 16 + 8 + 2 * k + 1] = t3; }
    }
  }
  __syncthreads();
  if (d < 64) {
    const int b = d >> 3, j = d & 7;
    float sg = 0.f, su = 0.f;
#pragma unroll
    for (int i = 0; i < 8; ++i) { sg += red[i][b * 16 + j]; su += red[i][b * 16 + 8 + j]; }
    sg = rbf(sg); su = rbf(su);
    const float sl = rbf(sg / (1.0f + __expf(-sg)));
    gu[b * F_ + j0 + j] = f2bf(sl * su);
  }
}

__global__ void __launch_bounds__(256) down_kernel(const unsigned short* __restrict__ gu, const unsigned short* __restrict__ wd,
                                                   const float* __restrict__ hbuf, unsigned short* __restrict__ out) {
  __shared__ float red[4][64];
  const int j0 = blockIdx.x * 8, t = threadIdx.x, lane = t & 63, w = t >> 6;
  float acc[64];
#pragma unroll
  for (int i = 0; i < 64; ++i) acc[i] = 0.f;
  for (int f = t; f < F_; f += 256) {
    const u32x4 wv = *reinterpret_cast<const u32x4*>(wd + (size_t)f * D_ + j0);
#pragma unroll
    for (int b = 0; b < 8; ++b) {
      const float gv = bflo(gu[b * F_ + f]);
#pragma unroll
      for (int k = 0; k < 4; ++k) { acc[b * 8 + 2 * k] = fmaf(gv, bflo(wv[k]), acc[b * 8 + 2 * k]); acc[b * 8 + 2 * k + 1] = fmaf(gv, bfhi(wv[k]), acc[b * 8 + 2 * k + 1]); }
    }
  }
#pragma unroll
  for (int i = 0; i < 64; ++i) { const float s = wave_sum(acc[i]); if (lane == 0) red[w][i] = s; }
  __syncthreads();
  if (t < 64) {
    const int b = t >> 3, j = t & 7;
    const float s = rbf(red[0][t] + red[1][t] + red[2][t] + red[3][t]);
    out[b * 512 + j0 + j] = f2bf(hbuf[b * 512 + j0 + j] + s);
  }
}

extern "C" void kernel_launch(void* const* d_in, const int* in_sizes, int n_in, void* d_out, int out_size, void* d_ws, size_t ws_size,
                              hipStream_t stream) {
  const unsigned short* x = (const unsigned short*)d_in[0];
  const unsigned short* kc = (const unsigned short*)d_in[1];
  const unsigned short* vc = (const unsigned short*)d_in[2];
  const unsigned short* wq = (const unsigned short*)d_in[3];
  const unsigned short* wo = (const unsigned short*)d_in[4];
  const unsigned short* wg = (const unsigned short*)d_in[6];
  const unsigned short* wu = (const unsigned short*)d_in[7];
  const unsigned short* wd = (const unsigned short*)d_in[8];
  char* ws = (char*)d_ws;
  float* partial = (float*)ws;
  float* hbuf = (float*)(ws + (1 << 20));
  unsigned short* hn = (unsigned short*)(ws + (1 << 20) + 65536);
  unsigned short* gu = (unsigned short*)(ws + (1 << 20) + 131072);
  attn_kernel<<<256, 1024, 0, stream>>>(x, kc, vc, wq, wo, partial);
  reduce_ln_kernel<<<8, 64, 0, stream>>>(x, partial, hbuf, hn);
  gateup_kernel<<<172, 512, 0, stream>>>(hn, wg, wu, gu);
  down_kernel<<<64, 256, 0, stream>>>(gu, wd, hbuf, (unsigned short*)d_out);
}
```

```cpp
#include <hip/hip_runtime.h>
#include <hip/hip_bf16.h>
#include <cstdint>

typedef __attribute__((ext_vector_type(4))) unsigned int u32x4;
typedef __attribute__((ext_vector_type(2))) unsigned int u32x2;
typedef __attribute__((ext_vector_type(4))) float f32x4;
typedef __attribute__((ext_vector_type(2))) __bf16 bf16x2;
typedef __attribute__((ext_vector_type(8))) __bf16 bf16x8;
typedef unsigned long long u64;
#define GAS __attribute__((address_space(1)))
typedef GAS u64 gu64;
#define RLX_AGENT __ATOMIC_RELAXED, __HIP_MEMORY_SCOPE_AGENT

constexpr int B_ = 8, D_ = 512, H_ = 32, SK = 4096, DH = 128, F_ = 1376;
constexpr int NW = 16;
#define RING 8
constexpr int U = RING;
constexpr int NIT = SK / (NW * U * 4);
constexpr int NC = F_ / 8;
constexpr int ND = D_ / 16;
constexpr int NKB = F_ / 32;

constexpr size_t WS_G = 0, WS_FLAGS = 4096, WS_HG = 8192, WS_GG = 32768, WS_PG = 131072, WS_DBG = 2u << 20;
constexpr u64 TAG = 1ull << 32;

__device__ __forceinline__ float bflo(unsigned v) { return __uint_as_float(v << 16); }
__device__ __forceinline__ float bfhi(unsigned v) { return __uint_as_float(v & 0xffff0000u); }
__device__ __forceinline__ float rbf(float f) { __bf16 b = (__bf16)f; return (float)b; }
__device__ __forceinline__ unsigned short f2bf(float f) { __bf16 b = (__bf16)f; return __builtin_bit_cast(unsigned short, b); }
__device__ __forceinline__ unsigned pack2(float lo, float hi) { return (unsigned)f2bf(lo) | ((unsigned)f2bf(hi) << 16); }
__device__ __forceinline__ float dot2(unsigned a, unsigned b, float acc) {
  return __builtin_amdgcn_fdot2_f32_bf16(__builtin_bit_cast(bf16x2, a), __builtin_bit_cast(bf16x2, b), acc, false);
}
template <int CTRL> __device__ __forceinline__ float dpp(float x) {
  return __builtin_bit_cast(float, __builtin_amdgcn_mov_dpp(__builtin_bit_cast(int, x), CTRL, 0xf, 0xf, true));
}
constexpr int XOR1 = 0xB1, XOR2 = 0x4E, XOR7 = 0x141, XOR8 = 0x128, ROR4 = 0x124;
__device__ __forceinline__ float xrow16_max(float x) {
  auto s = __builtin_amdgcn_permlane16_swap(__float_as_uint(x), __float_as_uint(x), false, false);
  x = fmaxf(__uint_as_float(s[0]), __uint_as_float(s[1]));
  auto t = __builtin_amdgcn_permlane32_swap(__float_as_uint(x), __float_as_uint(x), false, false);
  return fmaxf(__uint_as_float(t[0]), __uint_as_float(t[1]));
}
__device__ __forceinline__ float xrow16_sum(float x) {
  auto s = __builtin_amdgcn_permlane16_swap(__float_as_uint(x), __float_as_uint(x), false, false);
  x = __uint_as_float(s[0]) + __uint_as_float(s[1]);
  auto t = __builtin_amdgcn_permlane32_swap(__float_as_uint(x), __float_as_uint(x), false, false);
  return __uint_as_float(t[0]) + __uint_as_float(t[1]);
}
__device__ __forceinline__ float row16_sum(float x) {
  x += dpp<XOR8>(x); x += dpp<XOR7>(x); x += dpp<XOR2>(x); x += dpp<XOR1>(x); return x;
}
__device__ __forceinline__ float row16_max(float x) {
  x = fmaxf(x, dpp<XOR8>(x)); x = fmaxf(x, dpp<XOR7>(x)); x = fmaxf(x, dpp<XOR2>(x)); x = fmaxf(x, dpp<XOR1>(x)); return x;
}
__device__ __forceinline__ float wave_sum(float x) { return xrow16_sum(row16_sum(x)); }
__device__ __forceinline__ float wave_max(float x) { return xrow16_max(row16_max(x)); }
__device__ __forceinline__ u32x4 ldnt(const u32x4* p) { return __builtin_nontemporal_load(p); }
__device__ __forceinline__ u64 ld_g(gu64* p) { return __hip_atomic_load(p, RLX_AGENT); }
__device__ __forceinline__ void st_g(gu64* p, u64 v) { __hip_atomic_store(p, v, RLX_AGENT); }

__device__ __forceinline__ void ln_stats(const float (&v)[8], float& m_bf, float& sd_bf) {
  float s = 0.f;
#pragma unroll
  for (int i = 0; i < 8; ++i) s += v[i];
  s = wave_sum(s);
  const float mean = s * (1.0f / 512.0f);
  float q = 0.f;
#pragma unroll
  for (int i = 0; i < 8; ++i) { const float d = v[i] - mean; q = fmaf(d, d, q); }
  q = wave_sum(q);
  const float var = rbf(q * (1.0f / 512.0f));
  m_bf = rbf(mean);
  sd_bf = rbf(sqrtf(rbf(var + 1e-5f)));
}

constexpr int HN_STRIDE = 520;
struct Lds {
  float red[NW * 512];
  unsigned short wt[NKB * 64 * 8];
  unsigned short p[SK];
  unsigned short hn[8 * HN_STRIDE];
  unsigned q[64];
  float wmax[NW];
  float wsum[NW];
};

constexpr unsigned SPIN_CAP = 1u << 18;
#define STAMP(i)
#define STAMPW(i, wv)
#define STAMP_DECL
#define STAMP_FLUSH

__global__ void __launch_bounds__(1024) fused_kernel(const unsigned short* __restrict__ x, const unsigned short* __restrict__ kc,
                                                     const unsigned short* __restrict__ vc, const unsigned short* __restrict__ wq,
                                                     const unsigned short* __restrict__ wo, const unsigned short* __restrict__ wg,
                                                     const unsigned short* __restrict__ wu, const unsigned short* __restrict__ wd,
                                                     unsigned short* __restrict__ out, unsigned char* ws) {
  __shared__ __attribute__((aligned(16))) Lds L;
  const int bid = blockIdx.x;
  const int b = bid >> 5, h = (bid & 7) * 4 + ((bid >> 3) & 3);
  const int tid = threadIdx.x, lane = tid & 63, w = __builtin_amdgcn_readfirstlane(tid >> 6), r = lane >> 4, c = lane & 15;
  const size_t bh = (size_t)b * H_ + h;
  const u32x4* kp = reinterpret_cast<const u32x4*>(kc + bh * SK * DH);
  const u32x4* vp = reinterpret_cast<const u32x4*>(vc + bh * SK * DH);
  gu64* const Gw = (gu64*)(ws + WS_G);
  gu64* const FLAGS = (gu64*)(ws + WS_FLAGS);
  gu64* const HG = (gu64*)(ws + WS_HG);
  gu64* const GG = (gu64*)(ws + WS_GG);
  gu64* const PG = (gu64*)(ws + WS_PG);
  const bool isC = bid < NC, isD = (bid >= NC) && (bid < NC + ND);
  const int didx = bid - NC;
  STAMP_DECL
  STAMP(0);

  gu64* const pg_out = PG + (((size_t)b * 32 + (tid >> 4)) * 32 + h) * 16 + (tid & 15);
  if (tid < 512) st_g(pg_out, 0ull);
  else if (tid < 520) st_g(HG + b * 256 + 8 * h + (tid - 512), 0ull);
  else if (isC && tid >= 576 && tid < 608) st_g(GG + (((bid >> 2) * 4 + ((tid - 576) >> 3)) * 4 + (bid & 3)) * 8 + (tid & 7), 0ull);
  const u64 E = ld_g(Gw) + 1ull;

  const u32x4 xs = *reinterpret_cast<const u32x4*>(x + b * D_ + lane * 8);
  const u32x4 xr = *reinterpret_cast<const u32x4*>(x + b * D_ + w * 32 + r * 8);
  u32x4 wqv[8];
#pragma unroll
  for (int i = 0; i < 8; ++i)
    wqv[i] = *reinterpret_cast<const u32x4*>(wq + (size_t)(w * 32 + r * 8 + i) * (H_ * DH) + h * DH + c * 8);
  u32x4 tw[3];
  tw[0] = tw[1] = tw[2] = u32x4{0u, 0u, 0u, 0u};
  if (isC) tw[0] = *reinterpret_cast<const u32x4*>(((tid >> 9) ? wu : wg) + (size_t)(tid & 511) * F_ + 8 * bid);
  if (isD) {
#pragma unroll
    for (int i = 0; i < 3; ++i) { const int item = tid + 1024 * i; if (item < 2 * F_) tw[i] = *reinterpret_cast<const u32x4*>(wd + (size_t)(item >> 1) * D_ + 16 * didx + 8 * (item & 1)); }
  }
  asm volatile("" ::: "memory");
  u32x4 buf[U];
#pragma unroll
  for (int u = 0; u < U; ++u) buf[u] = ldnt(kp + (size_t)w * U * 64 + u * 64 + lane);

  {
    float xv[8];
#pragma unroll
    for (int k = 0; k < 4; ++k) { xv[2 * k] = bflo(xs[k]); xv[2 * k + 1] = bfhi(xs[k]); }
    float m_bf, sd_bf;
    ln_stats(xv, m_bf, sd_bf);
    float ln[8];
#pragma unroll
    for (int k = 0; k < 4; ++k) { ln[2 * k] = rbf(rbf(bflo(xr[k]) - m_bf) / sd_bf); ln[2 * k + 1] = rbf(rbf(bfhi(xr[k]) - m_bf) / sd_bf); }
    float qa[8];
#pragma unroll
    for (int j = 0; j < 8; ++j) qa[j] = 0.f;
#pragma unroll
    for (int i = 0; i < 8; ++i)
#pragma unroll
      for (int k = 0; k < 4; ++k) { qa[2 * k] = fmaf(ln[i], bflo(wqv[i][k]), qa[2 * k]); qa[2 * k + 1] = fmaf(ln[i], bfhi(wqv[i][k]), qa[2 * k + 1]); }
#pragma unroll
    for (int j = 0; j < 8; ++j) qa[j] = xrow16_sum(qa[j]);
    if (r == 0) {
      *reinterpret_cast<f32x4*>(&L.red[w * 128 + c * 8]) = f32x4{qa[0], qa[1], qa[2], qa[3]};
      *reinterpret_cast<f32x4*>(&L.red[w * 128 + c * 8 + 4]) = f32x4{qa[4], qa[5], qa[6], qa[7]};
    }
  }
  if (isC) {
    const int d = tid & 511, kb = d >> 5, kk = d & 31, nb = (tid >> 9) * 8;
    unsigned short* dst = &L.wt[(kb * 64 + (kk >> 3) * 16 + nb) * 8 + (kk & 7)];
#pragma unroll
    for (int k = 0; k < 4; ++k) { dst[(2 * k) * 8] = (unsigned short)(tw[0][k] & 0xffffu); dst[(2 * k + 1) * 8] = (unsigned short)(tw[0][k] >> 16); }
  }
  if (isD) {
#pragma unroll
    for (int i = 0; i < 3; ++i) {
      const int item = tid + 1024 * i;
      if (item < 2 * F_) {
        const int kf = item >> 1, kb = kf >> 5, kk = kf & 31, nb = (item & 1) * 8;
        unsigned short* dst = &L.wt[(kb * 64 + (kk >> 3) * 16 + nb) * 8 + (kk & 7)];
#pragma unroll
        for (int k = 0; k < 4; ++k) { dst[(2 * k) * 8] = (unsigned short)(tw[i][k] & 0xffffu); dst[(2 * k + 1) * 8] = (unsigned short)(tw[i][k] >> 16); }
      }
    }
  }
  asm volatile("s_waitcnt vmcnt(0)" ::: "memory");
  __syncthreads();
  if (tid == 0) st_g(FLAGS + bid, E);
  if (tid < 64) {
    float s0 = 0.f, s1 = 0.f;
#pragma unroll
    for (int i = 0; i < NW; ++i) { s0 += L.red[i * 128 + 2 * tid]; s1 += L.red[i * 128 + 2 * tid + 1]; }
    L.q[tid] = pack2(s0, s1);
  }
  __syncthreads();
  const u32x4 q = *reinterpret_cast<const u32x4*>(&L.q[c * 4]);
  STAMP(1);

  float lmax = -INFINITY;
  float* scf = L.red + 2048;
  for (int it = 0; it < NIT; ++it) {
    const u32x4* nxt = (it + 1 < NIT) ? kp + (size_t)((it + 1) * NW + w) * U * 64 + lane : vp + (size_t)w * U * 64 + lane;
    float xx[U];
#pragma unroll
    for (int u = 0; u < U; ++u) {
      float d = 0.f;
#pragma unroll
      for (int k = 0; k < 4; ++k) d = dot2(buf[u][k], q[k], d);
      xx[u] = d;
      buf[u] = ldnt(nxt + u * 64);
    }
#define TR_STEP(HALF, CTRL, BIT) _Pragma("unroll") for (int hh = 0; hh < (HALF); ++hh) { \
      const float lo_ = xx[hh], hi_ = xx[hh + (HALF)]; const float t1_ = lo_ + dpp<CTRL>(lo_), t2_ = hi_ + dpp<CTRL>(hi_); \
      xx[hh] = (c & (BIT)) ? t2_ : t1_; }
    int uidx;
    if constexpr (U == 16) { TR_STEP(8, XOR8, 8) TR_STEP(4, XOR7, 4) TR_STEP(2, XOR2, 2) TR_STEP(1, XOR1, 1) uidx = c; }
    else if constexpr (U == 8) { TR_STEP(4, XOR8, 8) TR_STEP(2, XOR7, 4) TR_STEP(1, XOR2, 2) xx[0] += dpp<XOR1>(xx[0]); uidx = c >> 1; }
    else { TR_STEP(2, XOR8, 8) TR_STEP(1, XOR7, 4) xx[0] += dpp<XOR2>(xx[0]); xx[0] += dpp<XOR1>(xx[0]); uidx = c >> 2; }
#undef TR_STEP
    const float s = rbf(rbf(xx[0]) * 0.08838834764831845f);
    lmax = fmaxf(lmax, s);
    scf[(it * NW + w) * (4 * U) + r * U + uidx] = s;
  }
  STAMP(2);
  u64 flagv = 0;
  if (w < 4) flagv = ld_g(FLAGS + tid);
  lmax = wave_max(lmax);
  if (lane == 0) L.wmax[w] = lmax;
  __syncthreads();
  float M = L.wmax[0];
#pragma unroll
  for (int i = 1; i < NW; ++i) M = fmaxf(M, L.wmax[i]);
  {
    constexpr int PER = 4 * U;
    const int idx = lane * 4;
    const int it = idx / PER, off = idx % PER;
    const f32x4 sv = *reinterpret_cast<const f32x4*>(&scf[(it * NW + w) * PER + off]);
    float e[4];
    float ls = 0.f;
#pragma unroll
    for (int i = 0; i < 4; ++i) { e[i] = __expf(sv[i] - M); ls += e[i]; }
    ls = wave_sum(ls);
    if (lane == 0) L.wsum[w] = ls;
    __syncthreads();
    float T = 0.f;
#pragma unroll
    for (int i = 0; i < NW; ++i) T += L.wsum[i];
    u32x2 pk; pk[0] = pack2(e[0] / T, e[1] / T); pk[1] = pack2(e[2] / T, e[3] / T);
    *reinterpret_cast<u32x2*>(&L.p[(it * NW + w) * PER + off]) = pk;
  }
  STAMP(3);
  float acc[8];
#pragma unroll
  for (int j = 0; j < 8; ++j) acc[j] = 0.f;
  const u32x4* wop = reinterpret_cast<const u32x4*>(wo + (size_t)(h * DH + 8 * w) * D_) + lane;
  u32x4 wov[8];
#define V_ITER(LAST) { \
    unsigned pp[U / 2]; \
    { const unsigned* src = reinterpret_cast<const unsigned*>(&L.p[(it * NW + w) * (4 * U) + r * U]); \
      _Pragma("unroll") for (int i = 0; i < U / 2; i += 2) { const u32x2 t = *reinterpret_cast<const u32x2*>(src + i); pp[i] = t[0]; pp[i + 1] = t[1]; } } \
    const u32x4* nxt = vp + (size_t)((it + 1) * NW + w) * U * 64 + lane; \
    _Pragma("unroll") for (int u = 0; u < U; u += 2) { \
      _Pragma("unroll") for (int k = 0; k < 4; ++k) { \
        const unsigned lo = __builtin_amdgcn_perm(buf[u + 1][k], buf[u][k], 0x05040100u); \
        const unsigned hi = __builtin_amdgcn_perm(buf[u + 1][k], buf[u][k], 0x07060302u); \
        acc[2 * k] = dot2(lo, pp[u / 2], acc[2 * k]); \
        acc[2 * k + 1] = dot2(hi, pp[u / 2], acc[2 * k + 1]); } \
      if (!(LAST)) { buf[u] = ldnt(nxt + u * 64); buf[u + 1] = ldnt(nxt + (u + 1) * 64); } \
    } }
  {
    int it = 0;
    for (; it < NIT - 1; ++it) V_ITER(false)
#pragma unroll
    for (int i = 0; i < 8; ++i) wov[i] = wop[i * 64];
    V_ITER(true)
  }
#undef V_ITER
  const float xval = bflo(x[b * D_ + 16 * h + c]);
  STAMP(4);
#pragma unroll
  for (int j = 0; j < 8; ++j) acc[j] = xrow16_sum(acc[j]);
  if (r == 0) {
    *reinterpret_cast<f32x4*>(&L.red[w * 128 + c * 8]) = f32x4{acc[0], acc[1], acc[2], acc[3]};
    *reinterpret_cast<f32x4*>(&L.red[w * 128 + c * 8 + 4]) = f32x4{acc[4], acc[5], acc[6], acc[7]};
  }
  if (w < 4) {
    for (unsigned sp = 0; !__all(flagv == E) && sp < SPIN_CAP; ++sp) { __builtin_amdgcn_s_sleep(2); flagv = ld_g(FLAGS + tid); }
  }
  __syncthreads();
  float ov;
  {
    const int d = 8 * w + (lane & 7), part = lane >> 3;
    ov = L.red[(2 * part) * 128 + d] + L.red[(2 * part + 1) * 128 + d];
    ov += dpp<XOR8>(ov);
    ov = xrow16_sum(ov);
    ov = rbf(ov);
  }
  __syncthreads();
  {
    float a2[8];
#pragma unroll
    for (int j = 0; j < 8; ++j) a2[j] = 0.f;
#pragma unroll
    for (int i = 0; i < 8; ++i) {
      const float o = __builtin_bit_cast(float, __builtin_amdgcn_readlane(__builtin_bit_cast(int, ov), i));
#pragma unroll
      for (int k = 0; k < 4; ++k) { a2[2 * k] = fmaf(o, bflo(wov[i][k]), a2[2 * k]); a2[2 * k + 1] = fmaf(o, bfhi(wov[i][k]), a2[2 * k + 1]); }
    }
    *reinterpret_cast<f32x4*>(&L.red[w * 512 + lane * 8]) = f32x4{a2[0], a2[1], a2[2], a2[3]};
    *reinterpret_cast<f32x4*>(&L.red[w * 512 + lane * 8 + 4]) = f32x4{a2[4], a2[5], a2[6], a2[7]};
  }
  __syncthreads();
  if (tid < 512) {
    float s = 0.f;
#pragma unroll
    for (int i = 0; i < NW; ++i) s += L.red[i * 512 + tid];
    st_g(pg_out, TAG | (u64)__float_as_uint(s));
  }
  STAMP(5);
  if (w == 15) {
    gu64* src = PG + (((size_t)b * 32 + h) * 32 + r) * 16 + c;
    float v[8];
    for (unsigned sp = 0;; ++sp) {
      bool ok = true;
#pragma unroll
      for (int i = 0; i < 8; ++i) { const u64 g = ld_g(src + i * 64); v[i] = __uint_as_float((unsigned)g); ok &= (g >> 32) == 1ull; }
      if (__all(ok) || sp >= SPIN_CAP) break;
      __builtin_amdgcn_s_sleep(2);
    }
    float s = 0.f;
#pragma unroll
    for (int i = 0; i < 8; ++i) s += v[i];
    s = xrow16_sum(s);
    const float hv = rbf(xval + rbf(s));
    const float hn_ = dpp<XOR1>(hv);
    if (r == 0 && (c & 1) == 0) st_g(HG + b * 256 + 8 * h + (c >> 1), TAG | (u64)pack2(hv, hn_));
    STAMPW(6, 15);
  }
  if (isC || isD) __syncthreads();
  if (isC) {
    if (w < 8) {
      gu64* src = HG + w * 256 + lane;
      unsigned hv2[4];
      for (unsigned sp = 0;; ++sp) {
        bool ok = true;
#pragma unroll
        for (int i = 0; i < 4; ++i) { const u64 g = ld_g(src + 64 * i); hv2[i] = (unsigned)g; ok &= (g >> 32) == 1ull; }
        if (__all(ok) || sp >= SPIN_CAP) break;
        __builtin_amdgcn_s_sleep(2);
      }
      float hv[8];
#pragma unroll
      for (int k = 0; k < 4; ++k) { hv[2 * k] = bflo(hv2[k]); hv[2 * k + 1] = bfhi(hv2[k]); }
      float m_bf, sd_bf;
      ln_stats(hv, m_bf, sd_bf);
#pragma unroll
      for (int k = 0; k < 4; ++k)
        *reinterpret_cast<unsigned*>(&L.hn[w * HN_STRIDE + 128 * k + 2 * lane]) = pack2(rbf(hv[2 * k] - m_bf) / sd_bf, rbf(hv[2 * k + 1] - m_bf) / sd_bf);
    }
    STAMP(7);
    __syncthreads();
    {
      u32x4 a = u32x4{0u, 0u, 0u, 0u};
      if (c < 8) a = *reinterpret_cast<const u32x4*>(&L.hn[c * HN_STRIDE + 32 * w + 8 * r]);
      const u32x4 bfr = *reinterpret_cast<const u32x4*>(&L.wt[(w * 64 + lane) * 8]);
      f32x4 cc = f32x4{0.f, 0.f, 0.f, 0.f};
      cc = __builtin_amdgcn_mfma_f32_16x16x32_bf16(__builtin_bit_cast(bf16x8, a), __builtin_bit_cast(bf16x8, bfr), cc, 0, 0, 0);
      if (r < 2) {
#pragma unroll
        for (int j = 0; j < 4; ++j) L.red[w * 128 + (4 * r + j) * 16 + c] = cc[j];
      }
    }
    __syncthreads();
    if (tid < 128) {
      float s = 0.f;
#pragma unroll
      for (int i = 0; i < NW; ++i) s += L.red[i * 128 + tid];
      s = rbf(s);
      const float up = dpp<XOR8>(s);
      const float sl = rbf(s / (1.0f + __expf(-s)));
      const float gu = rbf(sl * up);
      const float gn = dpp<XOR1>(gu);
      if (c < 8 && (c & 1) == 0) st_g(GG + (((bid >> 2) * 4 + (c >> 1)) * 4 + (bid & 3)) * 8 + (tid >> 4), TAG | (u64)pack2(gu, gn));
    }
    STAMP(8);
  } else if (isD) {
    float hres = 0.f;
    if (tid < 128) {
      gu64* src = HG + (tid >> 4) * 256 + 8 * didx + (c >> 1);
      u64 g = ld_g(src);
      for (unsigned sp = 0; !__all((g >> 32) == 1ull) && sp < SPIN_CAP; ++sp) { __builtin_amdgcn_s_sleep(1); g = ld_g(src); }
      hres = (c & 1) ? bfhi((unsigned)g) : bflo((unsigned)g);
    }
    __syncthreads();
    u32x4 a[3];
    {
      gu64* src = GG + (size_t)w * 128 + r * 8 + (c & 7);
      for (unsigned sp = 0;; ++sp) {
        bool ok = true;
#pragma unroll
        for (int i = 0; i < 3; ++i) {
          if (w + 16 * i < NKB) {
#pragma unroll
            for (int k = 0; k < 4; ++k) { const u64 g = ld_g(src + 2048 * i + 32 * k); a[i][k] = (unsigned)g; ok &= (g >> 32) == 1ull; }
          } else a[i] = u32x4{0u, 0u, 0u, 0u};
        }
        if (__all(ok) || sp >= SPIN_CAP) break;
        __builtin_amdgcn_s_sleep(4);
      }
    }
    STAMP(7);
    f32x4 cc = f32x4{0.f, 0.f, 0.f, 0.f};
#pragma unroll
    for (int i = 0; i < 3; ++i) {
      if (w + 16 * i < NKB) {
        const u32x4 bfr = *reinterpret_cast<const u32x4*>(&L.wt[((w + 16 * i) * 64 + lane) * 8]);
        cc = __builtin_amdgcn_mfma_f32_16x16x32_bf16(__builtin_bit_cast(bf16x8, a[i]), __builtin_bit_cast(bf16x8, bfr), cc, 0, 0, 0);
      }
    }
    __syncthreads();
    if (r < 2) {
#pragma unroll
      for (int j = 0; j < 4; ++j) L.red[w * 128 + (4 * r + j) * 16 + c] = cc[j];
    }
    __syncthreads();
    if (tid < 128) {
      float s = 0.f;
#pragma unroll
      for (int i = 0; i < NW; ++i) s += L.red[i * 128 + tid];
      out[(tid >> 4) * D_ + 16 * didx + c] = f2bf(hres + rbf(s));
      if (didx == 0 && tid == 0) st_g(Gw, E);
    }
    STAMP(8);
  }
  STAMP_FLUSH
}

extern "C" void kernel_launch(void* const* d_in, const int* in_sizes, int n_in, void* d_out, int out_size, void* d_ws, size_t ws_size,
                              hipStream_t stream) {
  fused_kernel<<<256, 1024, 0, stream>>>((const unsigned short*)d_in[0], (const unsigned short*)d_in[1], (const unsigned short*)d_in[2],
                                         (const unsigned short*)d_in[3], (const unsigned short*)d_in[4], (const unsigned short*)d_in[6],
                                         (const unsigned short*)d_in[7], (const unsigned short*)d_in[8], (unsigned short*)d_out,
                                         (unsigned char*)d_ws);
}
```

```cpp
#include <hip/hip_runtime.h>
#include <hip/hip_bf16.h>
#include <cstdint>

typedef __attribute__((ext_vector_type(4))) unsigned int u32x4;
typedef __attribute__((ext_vector_type(2))) unsigned int u32x2;
typedef __attribute__((ext_vector_type(4))) float f32x4;
typedef __attribute__((ext_vector_type(2))) __bf16 bf16x2;
typedef __attribute__((ext_vector_type(8))) __bf16 bf16x8;
typedef unsigned long long u64;
#define GAS __attribute__((address_space(1)))
typedef GAS u64 gu64;
#define RLX_AGENT __ATOMIC_RELAXED, __HIP_MEMORY_SCOPE_AGENT

constexpr int B_ = 8, D_ = 512, H_ = 32, SK = 4096, DH = 128, F_ = 1376;
constexpr int NW = 16;
#define RING 4
constexpr int U = RING;
constexpr int NIT = SK / (NW * U * 4);
constexpr int NC = F_ / 32;
constexpr int ND = D_ / 16;
constexpr int NKB = F_ / 32;

constexpr size_t WS_G = 0, WS_FLAGS = 4096, WS_HG = 8192, WS_GG = 32768, WS_PG = 131072, WS_QG = 1310720, WS_PROG = 1572864, WS_WTC = 4u << 20, WS_WTD = 8u << 20, WS_DBG = 2u << 20;
constexpr u64 TAG = 1ull << 32;

__device__ __forceinline__ float bflo(unsigned v) { return __uint_as_float(v << 16); }
__device__ __forceinline__ float bfhi(unsigned v) { return __uint_as_float(v & 0xffff0000u); }
__device__ __forceinline__ float rbf(float f) { __bf16 b = (__bf16)f; return (float)b; }
__device__ __forceinline__ unsigned short f2bf(float f) { __bf16 b = (__bf16)f; return __builtin_bit_cast(unsigned short, b); }
__device__ __forceinline__ unsigned pack2(float lo, float hi) { return (unsigned)f2bf(lo) | ((unsigned)f2bf(hi) << 16); }
__device__ __forceinline__ float dot2(unsigned a, unsigned b, float acc) {
  return __builtin_amdgcn_fdot2_f32_bf16(__builtin_bit_cast(bf16x2, a), __builtin_bit_cast(bf16x2, b), acc, false);
}
template <int CTRL> __device__ __forceinline__ float dpp(float x) {
  return __builtin_bit_cast(float, __builtin_amdgcn_mov_dpp(__builtin_bit_cast(int, x), CTRL, 0xf, 0xf, true));
}
constexpr int XOR1 = 0xB1, XOR2 = 0x4E, XOR7 = 0x141, XOR8 = 0x128, ROR4 = 0x124;
__device__ __forceinline__ float xrow16_max(float x) {
  auto s = __builtin_amdgcn_permlane16_swap(__float_as_uint(x), __float_as_uint(x), false, false);
  x = fmaxf(__uint_as_float(s[0]), __uint_as_float(s[1]));
  auto t = __builtin_amdgcn_permlane32_swap(__float_as_uint(x), __float_as_uint(x), false, false);
  return fmaxf(__uint_as_float(t[0]), __uint_as_float(t[1]));
}
__device__ __forceinline__ float xrow16_sum(float x) {
  auto s = __builtin_amdgcn_permlane16_swap(__float_as_uint(x), __float_as_uint(x), false, false);
  x = __uint_as_float(s[0]) + __uint_as_float(s[1]);
  auto t = __builtin_amdgcn_permlane32_swap(__float_as_uint(x), __float_as_uint(x), false, false);
  return __uint_as_float(t[0]) + __uint_as_float(t[1]);
}
__device__ __forceinline__ float row16_sum(float x) {
  x += dpp<XOR8>(x); x += dpp<XOR7>(x); x += dpp<XOR2>(x); x += dpp<XOR1>(x); return x;
}
__device__ __forceinline__ float row16_max(float x) {
  x = fmaxf(x, dpp<XOR8>(x)); x = fmaxf(x, dpp<XOR7>(x)); x = fmaxf(x, dpp<XOR2>(x)); x = fmaxf(x, dpp<XOR1>(x)); return x;
}
__device__ __forceinline__ float wave_sum(float x) { return xrow16_sum(row16_sum(x)); }
__device__ __forceinline__ float wave_max(float x) { return xrow16_max(row16_max(x)); }
__device__ __forceinline__ u32x4 ldnt(const u32x4* p) { return __builtin_nontemporal_load(p); }
__device__ __forceinline__ u64 ld_g(gu64* p) { return __hip_atomic_load(p, RLX_AGENT); }
__device__ __forceinline__ void st_g(gu64* p, u64 v) { __hip_atomic_store(p, v, RLX_AGENT); }

__device__ __forceinline__ void ln_stats(const float (&v)[8], float& m_bf, float& sd_bf) {
  float s = 0.f;
#pragma unroll
  for (int i = 0; i < 8; ++i) s += v[i];
  s = wave_sum(s);
  const float mean = s * (1.0f / 512.0f);
  float q = 0.f;
#pragma unroll
  for (int i = 0; i < 8; ++i) { const float d = v[i] - mean; q = fmaf(d, d, q); }
  q = wave_sum(q);
  const float var = rbf(q * (1.0f / 512.0f));
  m_bf = rbf(mean);
  sd_bf = rbf(sqrtf(rbf(var + 1e-5f)));
}

constexpr int HN_STRIDE = 520;
struct LdsTail {
  unsigned short wt[32768];
  unsigned short p[SK];
  unsigned short hn[8 * HN_STRIDE];
};
struct Lds {
  float red[6144];
  union { struct { unsigned q[64]; float wmax[NW]; float wsum[NW]; }; float small[2048]; };
  union { u32x4 pf[NW * 2 * U * 64]; LdsTail t; };
};
static_assert(sizeof(Lds) == 163840, "LDS budget");

constexpr unsigned SPIN_CAP = 1u << 18;
#define Q_OK(v) ((v[0] & v[1]) != 0xffffffffu && (v[2] & v[3]) != 0xffffffffu)
#define BSLP 1
#define DSLP 1
#define PSLP 1
#define DPIPE 1
#define VNT 1
#define LDV(p) ldnt(p)
#define NTW 0
#define LDW(p) (*(p))
#define LDQ(p) (*(p))
#define CPST "global_store_dwordx4 %0, %1, off sc1\n\ts_nop 1"
#define EARLY 0
#define PD 4
#define NOUNROLL
#define LOOP_PRAGMA _Pragma("unroll 1")
#define SKEW 6
#define LEAD 3
#define WOSP 3
#define TPF 0
#define SSH 5
#define UNP 0
#define TAU 227
#define TC0 300
#define TAU2 220
#define TJ1 26
#define BKL 0
#define BKB 0
#define BKM 4
#define BKJ 6
#define BKE 28
#define SKEWCTL2(it, PV) { \
    if (w == 15 && STEP0 + (it) + 2 < 2 * NIT - UNP) { \
      const int j_ = STEP0 + (it) + 2, g_ = j_ > 23 ? (j_ - 23) * TPF : 0; \
      u64 due_ = t_pace + (u64)(long long)((j_ < TJ1 ? j_ * TAU : TJ1 * TAU + (j_ - TJ1) * TAU2) - ((pdev * g_) >> 7)); \
      if (BKL > 0 && j_ >= BKJ && j_ <= BKE) { const u64 now_ = __builtin_amdgcn_s_memrealtime(); if (now_ > due_ + (u64)BKL) { const u64 sh_ = ((now_ - due_) * BKM >> 2) + (u64)BKB; t_pace += sh_; due_ += sh_; } } \
      _Pragma("unroll 1") for (unsigned sp_ = 0; sp_ < 256u && __builtin_amdgcn_s_memrealtime() < due_; ++sp_) __builtin_amdgcn_s_sleep(1); \
    } \
    __builtin_amdgcn_s_barrier(); }
#define STAMP(i)
#define STAMPW(i, wv)
#define STAMP_DECL
#define STAMP_FLUSH
#define STAMPP(i)
#define STAMPN(i) STAMP(i)

__global__ void __launch_bounds__(1024) fused_kernel(const unsigned short* __restrict__ x, const unsigned short* __restrict__ kc,
                                                     const unsigned short* __restrict__ vc, const unsigned short* __restrict__ wq,
                                                     const unsigned short* __restrict__ wo, const unsigned short* __restrict__ wg,
                                                     const unsigned short* __restrict__ wu, const unsigned short* __restrict__ wd,
                                                     unsigned short* __restrict__ out, unsigned char* ws) {
  __shared__ __attribute__((aligned(16))) Lds L;
  const int bid = blockIdx.x;
#define ROT 0
  const int b = bid >> 5, h = (((bid & 7) + ROT) & 7) * 4 + ((bid >> 3) & 3);
  const int tid = threadIdx.x, lane = tid & 63, w = __builtin_amdgcn_readfirstlane(tid >> 6), r = lane >> 4, c = lane & 15;
  const size_t bh = (size_t)b * H_ + h;
  const u32x4* kp = reinterpret_cast<const u32x4*>(kc + bh * SK * DH);
  const u32x4* vp = reinterpret_cast<const u32x4*>(vc + bh * SK * DH);
  gu64* const Gw = (gu64*)(ws + WS_G);
  gu64* const FLAGS = (gu64*)(ws + WS_FLAGS);
  gu64* const HG = (gu64*)(ws + WS_HG);
  gu64* const GG = (gu64*)(ws + WS_GG);
  gu64* const PG = (gu64*)(ws + WS_PG);
  gu64* const QG = (gu64*)(ws + WS_QG);
#define STAG 1
  u64 t_pace = __builtin_amdgcn_s_memrealtime() + (u64)(long long)(TC0 - (((bid >> 3) * TAU) >> SSH));
  const int pdev = ((31 * TAU) >> (SSH + 1)) - (((bid >> 3) * TAU) >> SSH);
  const int slot = bid >> 3;
  const int cidx = (bid & 7) * 6 + (slot - 8), didx = (bid & 7) * 4 + (slot - 14);
  const bool isQ = slot < 8, isC = slot >= 8 && slot < 14 && cidx < NC, isD = slot >= 14 && slot < 18;
  const int qhead = (bid & 7) * 4 + (slot >> 1), qhalf = slot & 1;
  STAMP_DECL
  STAMP(0);
#define CPF 0

  const u32x4* const kw = kp + (size_t)w * U * 64 + lane;
  const u32x4* const vw = vp + (size_t)w * U * 64 + lane;
  constexpr size_t CH = (size_t)NW * U * 64;
  u32x4 bufA[U], bufB[U];
#define RING_LD(i) { if ((i) < U) bufA[(i) % U] = ldnt(kw + ((i) % U) * 64); else bufB[(i) % U] = ldnt(kw + CH + ((i) % U) * 64); }
#pragma unroll
  for (int i = 0; i < EARLY; ++i) RING_LD(i)
  asm volatile("" ::: "memory");
  gu64* const pg_out = PG + (((size_t)b * 32 + (tid >> 4)) * 32 + h) * 8 + ((tid & 15) >> 1);
#define CLEAR_OUTBOXES \
  if (w == 15) { \
_Pragma("unroll") \
    for (int i = 0; i < 4; ++i) st_g(PG + (((size_t)b * 32 + 8 * i + (lane >> 3)) * 32 + h) * 8 + (lane & 7), ~0ull); \
    if (lane < 4) st_g(HG + b * 128 + 4 * h + lane, ~0ull); \
    if (isC) st_g(GG + cidx * 64 + lane, ~0ull); \
  } \

#define LATECLR 0
  CLEAR_OUTBOXES
  u32x4 xs = u32x4{0u, 0u, 0u, 0u}, xa = u32x4{0u, 0u, 0u, 0u};
  u32x2 wqs[8];
#pragma unroll
  for (int k = 0; k < 8; ++k) wqs[k] = u32x2{0u, 0u};
  if (isQ) {
    xs = *reinterpret_cast<const u32x4*>(x + (w & 7) * D_ + lane * 8);
    xa = *reinterpret_cast<const u32x4*>(x + (c & 7) * D_ + w * 32 + r * 8);
    const unsigned short* src = wq + (size_t)(w * 32 + r * 8) * (H_ * DH) + qhead * DH + 64 * qhalf + 4 * c;
#pragma unroll
    for (int k = 0; k < 8; ++k) wqs[k] = LDQ(reinterpret_cast<const u32x2*>(src + (size_t)k * (H_ * DH)));
  }
  int zero_ = 0;
  asm volatile("" : "+v"(zero_) :: "memory");
  const u64 E = ld_g(Gw + zero_) + 1ull;
  asm volatile("" ::: "memory");
  __builtin_amdgcn_s_barrier();
  if (w == 15) asm volatile("s_waitcnt vmcnt(0)" ::: "memory");
  u32x4 tw[4];
  tw[0] = tw[1] = tw[2] = tw[3] = u32x4{0u, 0u, 0u, 0u};
#define TW_LOADS \
  if (isC) { \
    _Pragma("unroll") for (int j = 0; j < 4; ++j) \
      tw[j] = LDW(reinterpret_cast<const u32x4*>((j >> 1 ? wu : wg) + (size_t)((tid >> 2) + 256 * (j & 1)) * F_ + 32 * cidx + 8 * (tid & 3))); \
  } \
  if (isD) { \
    _Pragma("unroll") for (int i = 0; i < 3; ++i) \
      if (tid + 1024 * i < 2 * F_) tw[i] = LDW(reinterpret_cast<const u32x4*>(wd + (size_t)((tid + 1024 * i) >> 1) * D_ + 16 * didx + 8 * (tid & 1))); \
  } \
  asm volatile("" ::: "memory");
#define TWPOS 0
  const u32x4* wop = reinterpret_cast<const u32x4*>(wo + (size_t)(h * DH + 8 * w) * D_) + lane;
  u32x4 wov[8];
#define WO_LOADS _Pragma("unroll") for (int i = 0; i < 8; ++i) wov[i] = LDW(wop + i * 64); asm volatile("" ::: "memory");
#define WOPOS 9
  TW_LOADS
#pragma unroll
  for (int i = EARLY; i < PD; ++i) RING_LD(i)
  asm volatile("" ::: "memory");
  u64 qtag = (E * 0x9E3779B97F4A7C15ull) & 0xFFFFFFFFFFFFull;
  qtag ^= qtag >> 24;
  if (isQ) {
    float* const lnst = reinterpret_cast<float*>(&L.pf[0]);
    {
      float xv[8];
#pragma unroll
      for (int k = 0; k < 4; ++k) { xv[2 * k] = bflo(xs[k]); xv[2 * k + 1] = bfhi(xs[k]); }
      float m_bf, sd_bf;
      ln_stats(xv, m_bf, sd_bf);
      if (lane == 0 && w < 8) { lnst[w] = m_bf; lnst[8 + w] = sd_bf; }
    }
    STAMPP(2);
    __syncthreads();
    STAMP(11);
    {
      const float m_bf = lnst[c & 7], sd_bf = lnst[8 + (c & 7)];
      u32x4 a;
#pragma unroll
      for (int k = 0; k < 4; ++k) a[k] = pack2(rbf(bflo(xa[k]) - m_bf) / sd_bf, rbf(bfhi(xa[k]) - m_bf) / sd_bf);
      f32x4 cc[4];
#pragma unroll
      for (int j = 0; j < 4; ++j) {
        u32x4 wqf;
#pragma unroll
        for (int k = 0; k < 4; ++k) wqf[k] = __builtin_amdgcn_perm(wqs[2 * k + 1][j >> 1], wqs[2 * k][j >> 1], (j & 1) ? 0x07060302u : 0x05040100u);
        cc[j] = __builtin_amdgcn_mfma_f32_16x16x32_bf16(__builtin_bit_cast(bf16x8, a), __builtin_bit_cast(bf16x8, wqf), f32x4{0.f, 0.f, 0.f, 0.f}, 0, 0, 0);
      }
      if (r < 2) {
#pragma unroll
        for (int i = 0; i < 4; ++i) *reinterpret_cast<f32x4*>(&L.red[w * 512 + (4 * r + i) * 64 + 4 * c]) = f32x4{cc[0][i], cc[1][i], cc[2][i], cc[3][i]};
      }
    }
    STAMPP(3);
    __syncthreads();
    if (tid < 512) {
      float s = 0.f;
#pragma unroll
      for (int i = 0; i < NW; ++i) s += L.red[i * 512 + tid];
      st_g(QG + ((size_t)(tid >> 6) * 32 + qhead) * 128 + 64 * qhalf + lane, (qtag << 16) | (u64)f2bf(s));
    }
    __syncthreads();
  }
  STAMPP(4);
#pragma unroll
  for (int i = (PD > EARLY ? PD : EARLY); i < 2 * U; ++i) {
    asm volatile("s_waitcnt vmcnt(%0)" :: "n"(PD - 1) : "memory");
    RING_LD(i)
  }
#undef RING_LD
  u32x4* const pfw = &L.pf[w * 2 * U * 64 + lane];
  {
    gu64* const qsrc = QG + ((size_t)b * 32 + h) * 128 + 2 * lane;
    u64 g0 = 0, g1 = 0;
#pragma unroll
    for (int u = 0; u < U; ++u) pfw[u * 64] = bufA[u];
    if (w == 15) { g0 = ld_g(qsrc); g1 = ld_g(qsrc + 1); }
#pragma unroll
    for (int u = 0; u < U; ++u) bufA[u] = ldnt(kw + 2 * CH + u * 64);
#pragma unroll
    for (int u = 0; u < U; ++u) pfw[(U + u) * 64] = bufB[u];
    bool got = false;
    if (w == 15) {
      got = __all((g0 >> 16) == qtag && (g1 >> 16) == qtag);
      if (!got) { g0 = ld_g(qsrc); g1 = ld_g(qsrc + 1); }
    }
#pragma unroll
    for (int u = 0; u < U; ++u) bufB[u] = ldnt(kw + 3 * CH + u * 64);
    if (w == 15) {
      if (!got) got = __all((g0 >> 16) == qtag && (g1 >> 16) == qtag);
      for (unsigned sp = 0; !got && sp < SPIN_CAP; ++sp) {
        __builtin_amdgcn_s_sleep(1);
        g0 = ld_g(qsrc); g1 = ld_g(qsrc + 1);
        got = __all((g0 >> 16) == qtag && (g1 >> 16) == qtag);
      }
      L.q[lane] = (unsigned)(g0 & 0xffffu) | ((unsigned)(g1 & 0xffffu) << 16);
    }
    STAMP(10);
  }
  __syncthreads();
  const u32x4 q = *reinterpret_cast<const u32x4*>(&L.q[c * 4]);
  if (tid == 0) st_g(FLAGS + bid, E);
  STAMP(1);

#define STEP0 0
  float lmax = -INFINITY;
  float* scf = L.red + 2048;
#define TR_STEP(HALF, CTRL, BIT) _Pragma("unroll") for (int hh = 0; hh < (HALF); ++hh) { \
      const float lo_ = xx[hh], hi_ = xx[hh + (HALF)]; const float t1_ = lo_ + dpp<CTRL>(lo_), t2_ = hi_ + dpp<CTRL>(hi_); \
      xx[hh] = (c & (BIT)) ? t2_ : t1_; }
#define K_TAIL(IT) \
    TR_STEP(2, XOR8, 8) TR_STEP(1, XOR7, 4) xx[0] += dpp<XOR2>(xx[0]); xx[0] += dpp<XOR1>(xx[0]); \
    const float s = rbf(rbf(xx[0]) * 0.08838834764831845f); \
    lmax = fmaxf(lmax, s); \
    scf[((IT) * NW + w) * (4 * U) + r * U + (c >> 2)] = s;
#define K_STEP(BUF, IT, NXT) { \
    float xx[U]; \
    _Pragma("unroll") for (int u = 0; u < U; ++u) { \
      float d = 0.f; \
      _Pragma("unroll") for (int k = 0; k < 4; ++k) d = dot2(BUF[u][k], q[k], d); \
      xx[u] = d; } \
    asm volatile("" : "+v"(xx[0]), "+v"(xx[1]), "+v"(xx[2]), "+v"(xx[3])); \
    SKEWCTL2(IT, pv_##BUF) \
    _Pragma("unroll") for (int u = 0; u < U; ++u) BUF[u] = ldnt((NXT) + u * 64); \
    K_TAIL(IT) }
  static_assert(U == 4, "K_STEP is written for U == 4");
#pragma unroll
  for (int it = 0; it < 2; ++it) {
    float xx[U];
#pragma unroll
    for (int u = 0; u < U; ++u) {
      const u32x4 kv = pfw[(it * U + u) * 64];
      float d = 0.f;
#pragma unroll
      for (int k = 0; k < 4; ++k) d = dot2(kv[k], q[k], d);
      xx[u] = d;
    }
    K_TAIL(it)
  }
  K_STEP(bufA, 2, kw + (size_t)4 * CH)
  K_STEP(bufB, 3, kw + (size_t)5 * CH)
  if (isC) {
#pragma unroll
    for (int j = 0; j < 4; ++j) {
      const int row = (tid >> 2) + 256 * (j & 1), kb = row >> 5, kk = row & 31, t = (j >> 1) * 2 + ((tid >> 1) & 1), nb = (tid & 1) * 8;
      unsigned short* dst = &L.t.wt[((kb * 4 + t) * 64 + (kk >> 3) * 16 + nb) * 8 + (kk & 7)];
#pragma unroll
      for (int k = 0; k < 4; ++k) { dst[(2 * k) * 8] = (unsigned short)(tw[j][k] & 0xffffu); dst[(2 * k + 1) * 8] = (unsigned short)(tw[j][k] >> 16); }
    }
  }
  if (isD) {
#pragma unroll
    for (int i = 0; i < 3; ++i) {
      const int item = tid + 1024 * i;
      if (item < 2 * F_) {
        const int kf = item >> 1, kb = kf >> 5, kk = kf & 31, nb = (item & 1) * 8;
        unsigned short* dst = &L.t.wt[(kb * 64 + (kk >> 3) * 16 + nb) * 8 + (kk & 7)];
#pragma unroll
        for (int k = 0; k < 4; ++k) { dst[(2 * k) * 8] = (unsigned short)(tw[i][k] & 0xffffu); dst[(2 * k + 1) * 8] = (unsigned short)(tw[i][k] >> 16); }
      }
    }
  }
#define WO1(i) wov[i] = LDW(wop + (i) * 64); asm volatile("" ::: "memory");
  K_STEP(bufA, 4, kw + (size_t)6 * CH)
  K_STEP(bufB, 5, kw + (size_t)7 * CH)
  K_STEP(bufA, 6, kw + (size_t)8 * CH)
  K_STEP(bufB, 7, kw + (size_t)9 * CH)
  K_STEP(bufA, 8, kw + (size_t)10 * CH)
  WO1(0)
  K_STEP(bufB, 9, kw + (size_t)11 * CH)
  K_STEP(bufA, 10, kw + (size_t)12 * CH)
  WO1(1)
  K_STEP(bufB, 11, kw + (size_t)13 * CH)
  K_STEP(bufA, 12, kw + (size_t)14 * CH)
  WO1(2)
  K_STEP(bufB, 13, kw + (size_t)15 * CH)
  K_STEP(bufA, NIT - 2, vw)
  K_STEP(bufB, NIT - 1, vw + CH)
#undef K_TAIL
#undef K_STEP
#undef TR_STEP
  STAMPN(2);
  u64 flagv = 0;
  lmax = wave_max(lmax);
  if (lane == 0) L.wmax[w] = lmax;
  __syncthreads();
  float M = L.wmax[0];
#pragma unroll
  for (int i = 1; i < NW; ++i) M = fmaxf(M, L.wmax[i]);
  {
    constexpr int PER = 4 * U;
    const int idx = lane * 4;
    const int it = idx / PER, off = idx % PER;
    const f32x4 sv = *reinterpret_cast<const f32x4*>(&scf[(it * NW + w) * PER + off]);
    float e[4];
    float ls = 0.f;
#pragma unroll
    for (int i = 0; i < 4; ++i) { e[i] = __expf(sv[i] - M); ls += e[i]; }
    ls = wave_sum(ls);
    if (lane == 0) L.wsum[w] = ls;
    __syncthreads();
    float T = 0.f;
#pragma unroll
    for (int i = 0; i < NW; ++i) T += L.wsum[i];
    u32x2 pk; pk[0] = pack2(e[0] / T, e[1] / T); pk[1] = pack2(e[2] / T, e[3] / T);
    *reinterpret_cast<u32x2*>(&L.t.p[(it * NW + w) * PER + off]) = pk;
  }
  STAMPN(3);
#undef STEP0
#define STEP0 NIT
  float acc[8];
#pragma unroll
  for (int j = 0; j < 8; ++j) acc[j] = 0.f;
#define V_STEP(BUF, IT, LOADS) { \
    const u32x2 pp = *reinterpret_cast<const u32x2*>(&L.t.p[((IT) * NW + w) * (4 * U) + r * U]); \
    _Pragma("unroll") for (int u = 0; u < U; u += 2) { \
      _Pragma("unroll") for (int k = 0; k < 4; ++k) { \
        const unsigned lo = __builtin_amdgcn_perm(BUF[u + 1][k], BUF[u][k], 0x05040100u); \
        const unsigned hi = __builtin_amdgcn_perm(BUF[u + 1][k], BUF[u][k], 0x07060302u); \
        acc[2 * k] = dot2(lo, pp[u / 2], acc[2 * k]); \
        acc[2 * k + 1] = dot2(hi, pp[u / 2], acc[2 * k + 1]); } } \
    asm volatile("" : "+v"(acc[0]), "+v"(acc[1]), "+v"(acc[2]), "+v"(acc[3]), "+v"(acc[4]), "+v"(acc[5]), "+v"(acc[6]), "+v"(acc[7])); \
    SKEWCTL2(IT, pv_##BUF) \
    LOADS }
  V_STEP(bufA, 0, _Pragma("unroll") for (int u = 0; u < U; ++u) bufA[u] = LDV(vw + (size_t)2 * CH + u * 64);)
  V_STEP(bufB, 1, _Pragma("unroll") for (int u = 0; u < U; ++u) bufB[u] = LDV(vw + (size_t)3 * CH + u * 64);)
#define VLD(BUF, J) _Pragma("unroll") for (int u = 0; u < U; ++u) BUF[u] = LDV(vw + (size_t)(J) * CH + u * 64);
  WO1(3) V_STEP(bufA, 2, VLD(bufA, 4)) V_STEP(bufB, 3, VLD(bufB, 5)) WO1(4) V_STEP(bufA, 4, VLD(bufA, 6)) V_STEP(bufB, 5, VLD(bufB, 7))
  WO1(5) V_STEP(bufA, 6, VLD(bufA, 8)) V_STEP(bufB, 7, VLD(bufB, 9)) WO1(6) V_STEP(bufA, 8, VLD(bufA, 10)) V_STEP(bufB, 9, VLD(bufB, 11))
  WO1(7) V_STEP(bufA, 10, VLD(bufA, 12)) V_STEP(bufB, 11, VLD(bufB, 13)) V_STEP(bufA, 12, VLD(bufA, 14)) V_STEP(bufB, 13, VLD(bufB, 15))
#pragma unroll
  for (int i = 0; i < 8; i += 2) {
#pragma unroll
    for (int k = 0; k < 4; ++k) {
      const unsigned lo = __builtin_amdgcn_perm(wov[i + 1][k], wov[i][k], 0x05040100u);
      const unsigned hi = __builtin_amdgcn_perm(wov[i + 1][k], wov[i][k], 0x07060302u);
      wov[i][k] = lo; wov[i + 1][k] = hi;
    }
    asm volatile("" : "+v"(wov[i]), "+v"(wov[i + 1]));
  }
  if (w < 4) flagv = ld_g(FLAGS + tid);
  const u32x2 xval = *reinterpret_cast<const u32x2*>(x + b * D_ + 16 * h + 4 * (lane & 3));
  V_STEP(bufA, NIT - 2, )
  V_STEP(bufB, NIT - 1, )
#undef V_STEP
  __builtin_amdgcn_s_setprio(0);
  STAMPN(4);
#pragma unroll
  for (int j = 0; j < 8; ++j) acc[j] = xrow16_sum(acc[j]);
  float* const op = reinterpret_cast<float*>(&L.pf[5632]);
  static_assert(sizeof(LdsTail) <= 5632 * 16, "o partials overlap the tail buffers");
  if (r == 0) {
    *reinterpret_cast<f32x4*>(&op[w * 128 + c * 8]) = f32x4{acc[0], acc[1], acc[2], acc[3]};
    *reinterpret_cast<f32x4*>(&op[w * 128 + c * 8 + 4]) = f32x4{acc[4], acc[5], acc[6], acc[7]};
  }
  if (w < 4) {
    for (unsigned sp = 0; !__all(flagv == E) && sp < SPIN_CAP; ++sp) { __builtin_amdgcn_s_sleep(2); flagv = ld_g(FLAGS + tid); }
  }
  __syncthreads();
  float ov;
  {
    const int d = 8 * w + (lane & 7), part = lane >> 3;
    ov = op[(2 * part) * 128 + d] + op[(2 * part + 1) * 128 + d];
    ov += dpp<XOR8>(ov);
    ov = xrow16_sum(ov);
    ov = __uint_as_float(pack2(ov, dpp<XOR1>(ov)));
  }
  {
    float a2[8];
#pragma unroll
    for (int j = 0; j < 8; ++j) a2[j] = 0.f;
#pragma unroll
    for (int i = 0; i < 8; i += 2) {
      const unsigned o2 = (unsigned)__builtin_amdgcn_readlane(__builtin_bit_cast(int, ov), i);
#pragma unroll
      for (int k = 0; k < 4; ++k) { a2[2 * k] = dot2(wov[i][k], o2, a2[2 * k]); a2[2 * k + 1] = dot2(wov[i + 1][k], o2, a2[2 * k + 1]); }
    }
    *reinterpret_cast<f32x4*>(&L.red[w * 512 + lane * 8]) = f32x4{a2[0], a2[1], a2[2], a2[3]};
    *reinterpret_cast<f32x4*>(&L.red[w * 512 + lane * 8 + 4]) = f32x4{a2[4], a2[5], a2[6], a2[7]};
  }
  __syncthreads();
  if (tid < 512) {
    float s = 0.f;
#pragma unroll
    for (int i = 0; i < NW; ++i) s += L.red[i * 512 + tid];
    u64 pq = (u64)__float_as_uint(s) | ((u64)__float_as_uint(dpp<XOR1>(s)) << 32);
    if (pq == ~0ull) pq = 0x7FC000007FC00000ull;
    if ((tid & 1) == 0) st_g(pg_out, pq);
  }
  STAMP(5);
  if (w == 15) {
    const auto rs = __builtin_amdgcn_make_buffer_rsrc((void*)(ws + WS_PG + ((size_t)b * 32 + h) * 2048), 0, 2048, 0x00020000);
    u32x4 p0, p1;
    for (unsigned sp = 0;; ++sp) {
      asm volatile("" ::: "memory");
      p0 = __builtin_bit_cast(u32x4, __builtin_amdgcn_raw_buffer_load_b128(rs, lane * 16, 0, 16));
      p1 = __builtin_bit_cast(u32x4, __builtin_amdgcn_raw_buffer_load_b128(rs, lane * 16, 1024, 16));
      if (__all(Q_OK(p0) && Q_OK(p1)) || sp >= SPIN_CAP) break;
      __builtin_amdgcn_s_sleep(BSLP);
    }
    float hv[4];
#pragma unroll
    for (int j = 0; j < 4; ++j) {
      float s = __uint_as_float(p0[j]) + __uint_as_float(p1[j]);
      s += dpp<ROR4>(s); s += dpp<XOR8>(s);
      s = xrow16_sum(s);
      hv[j] = ((j & 1) ? bfhi(xval[j >> 1]) : bflo(xval[j >> 1])) + rbf(s);
    }
    const unsigned lo = pack2(hv[0], hv[1]), hi = pack2(hv[2], hv[3]);
    u64 hq = (u64)lo | ((u64)hi << 32);
    if (hq == ~0ull) hq = 0x7FC07FC07FC07FC0ull;
    if (lane < 4) st_g(HG + b * 128 + 4 * h + lane, hq);
  }
  if (isC || isD) __syncthreads();
  STAMP(6);
  if (isC) {
    if (w < 8) {
      const auto rs = __builtin_amdgcn_make_buffer_rsrc((void*)(ws + WS_HG), 0, 8192, 0x00020000);
      const int off = w * 1024 + lane * 16;
#define HG_LD __builtin_bit_cast(u32x4, __builtin_amdgcn_raw_buffer_load_b128(rs, off, 0, 16))
      u32x4 hv2;
      for (unsigned sp = 0;; ++sp) {
        asm volatile("" ::: "memory");
        hv2 = HG_LD;
        if (__all(Q_OK(hv2)) || sp >= SPIN_CAP) break;
        __builtin_amdgcn_s_sleep(PSLP);
      }
      float hv[8];
#pragma unroll
      for (int k = 0; k < 4; ++k) { hv[2 * k] = bflo(hv2[k]); hv[2 * k + 1] = bfhi(hv2[k]); }
      float m_bf, sd_bf;
      ln_stats(hv, m_bf, sd_bf);
      u32x4 hn4;
#pragma unroll
      for (int k = 0; k < 4; ++k) hn4[k] = pack2(rbf(hv[2 * k] - m_bf) / sd_bf, rbf(hv[2 * k + 1] - m_bf) / sd_bf);
      *reinterpret_cast<u32x4*>(&L.t.hn[w * HN_STRIDE + 8 * lane]) = hn4;
    }
    STAMP(7);
    __syncthreads();
    {
      const int t = w & 3, kq = w >> 2;
      f32x4 cc = f32x4{0.f, 0.f, 0.f, 0.f};
#pragma unroll
      for (int i = 0; i < 4; ++i) {
        const int kb = 4 * kq + i;
        u32x4 a = u32x4{0u, 0u, 0u, 0u};
        if (c < 8) a = *reinterpret_cast<const u32x4*>(&L.t.hn[c * HN_STRIDE + 32 * kb + 8 * r]);
        const u32x4 bfr = *reinterpret_cast<const u32x4*>(&L.t.wt[((kb * 4 + t) * 64 + lane) * 8]);
        cc = __builtin_amdgcn_mfma_f32_16x16x32_bf16(__builtin_bit_cast(bf16x8, a), __builtin_bit_cast(bf16x8, bfr), cc, 0, 0, 0);
      }
      if (r < 2) {
#pragma unroll
        for (int j = 0; j < 4; ++j) L.red[kq * 512 + (4 * r + j) * 64 + t * 16 + c] = cc[j];
      }
    }
    __syncthreads();
    if (tid < 512) {
      float s = (L.red[tid] + L.red[512 + tid]) + (L.red[1024 + tid] + L.red[1536 + tid]);
      s = rbf(s);
      const auto sw = __builtin_amdgcn_permlane32_swap(__float_as_uint(s), __float_as_uint(s), false, false);
      const float g = __uint_as_float(sw[0]), up = __uint_as_float(sw[1]);
      const float sl = rbf(g / (1.0f + __expf(-g)));
      const float gu = rbf(sl * up);
      const unsigned lo = pack2(gu, dpp<XOR1>(gu));
      const unsigned hi = __float_as_uint(dpp<XOR2>(__uint_as_float(lo)));
      u64 gq = (u64)lo | ((u64)hi << 32);
      if (gq == ~0ull) gq = 0x7FC07FC07FC07FC0ull;
      if (lane < 32 && (lane & 3) == 0) st_g(GG + (cidx * 8 + w) * 8 + (lane >> 2), gq);
    }
    STAMP(8);
  } else if (isD) {
    float hres = 0.f;
    if (tid < 128) {
      gu64* src = HG + (tid >> 4) * 128 + 4 * didx + (c >> 2);
      u64 g = ld_g(src);
      for (unsigned sp = 0; !__all(g != ~0ull) && sp < SPIN_CAP; ++sp) { __builtin_amdgcn_s_sleep(1); g = ld_g(src); }
      const unsigned gh = (c & 2) ? (unsigned)(g >> 32) : (unsigned)g;
      hres = (c & 1) ? bfhi(gh) : bflo(gh);
    }
    __syncthreads();
    STAMP(9);
    u32x4 a[3];
    {
      const auto rs = __builtin_amdgcn_make_buffer_rsrc((void*)(ws + WS_GG), 0, NKB * 512, 0x00020000);
      const int off = ((w * 8 + (c & 7)) * 8 + 2 * r) * 8;
      const int off2 = off + (w + 32 < NKB ? 32 * 512 : 0);
#define GG_LD(v) { v[0] = __builtin_bit_cast(u32x4, __builtin_amdgcn_raw_buffer_load_b128(rs, off, 0, 16)); \
        v[1] = __builtin_bit_cast(u32x4, __builtin_amdgcn_raw_buffer_load_b128(rs, off, 16 * 512, 16)); \
        v[2] = __builtin_bit_cast(u32x4, __builtin_amdgcn_raw_buffer_load_b128(rs, off2, 0, 16)); }
      for (unsigned sp = 0;; ++sp) {
        asm volatile("" ::: "memory");
        GG_LD(a)
        if (__all(Q_OK(a[0]) && Q_OK(a[1]) && Q_OK(a[2])) || sp >= SPIN_CAP) break;
        __builtin_amdgcn_s_sleep(DSLP);
      }
    }
    STAMP(7);
    f32x4 cc = f32x4{0.f, 0.f, 0.f, 0.f};
#pragma unroll
    for (int i = 0; i < 3; ++i) {
      if (w + 16 * i < NKB) {
        const u32x4 bfr = *reinterpret_cast<const u32x4*>(&L.t.wt[((w + 16 * i) * 64 + lane) * 8]);
        cc = __builtin_amdgcn_mfma_f32_16x16x32_bf16(__builtin_bit_cast(bf16x8, a[i]), __builtin_bit_cast(bf16x8, bfr), cc, 0, 0, 0);
      }
    }
    __syncthreads();
    if (r < 2) {
#pragma unroll
      for (int j = 0; j < 4; ++j) L.red[w * 128 + (4 * r + j) * 16 + c] = cc[j];
    }
    __syncthreads();
    if (tid < 128) {
      float s = 0.f;
#pragma unroll
      for (int i = 0; i < NW; ++i) s += L.red[i * 128 + tid];
      out[(tid >> 4) * D_ + 16 * didx + c] = f2bf(hres + rbf(s));
      if (didx == 0 && tid == 0) st_g(Gw, E);
    }
    STAMP(8);
  }
  STAMP_FLUSH
}

extern "C" void kernel_launch(void* const* d_in, const int* in_sizes, int n_in, void* d_out, int out_size, void* d_ws, size_t ws_size,
                              hipStream_t stream) {
  fused_kernel<<<256, 1024, 0, stream>>>((const unsigned short*)d_in[0], (const unsigned short*)d_in[1], (const unsigned short*)d_in[2],
                                         (const unsigned short*)d_in[3], (const unsigned short*)d_in[4], (const unsigned short*)d_in[6],
                                         (const unsigned short*)d_in[7], (const unsigned short*)d_in[8], (unsigned short*)d_out,
                                         (unsigned char*)d_ws);
}
```
